```python
import torch

def _softmax(x, axis=-1):
    return torch.softmax(x, dim=axis)

def _gelu(x):
    return torch.nn.functional.gelu(x, approximate='tanh')

def _silu(x):
    return torch.nn.functional.silu(x)

def _sigmoid(x):
    return torch.sigmoid(x)

def _layernorm(x, axis=-1, eps=1e-05):
    m = x.mean(dim=axis, keepdim=True)
    v = x.var(dim=axis, keepdim=True, unbiased=False)
    return (x - m) / torch.sqrt(v + eps)

def _scatter_add(tgt, idx, src, axis=0):
    return tgt.clone().index_add_(axis, idx, src)

def _segment_sum(src, seg, n):
    out = torch.zeros((n,) + src.shape[1:], dtype=src.dtype)
    return out.index_add_(0, seg, src)

def _pad(x, pw):
    flat = tuple((v for pair in reversed(pw) for v in pair))
    return torch.nn.functional.pad(x, flat)
kernel_name = 'adaLN_cond__compressed_kv_cla__dense_activation_squared_relu__decode'

def setup_inputs():

    _w = lambda a: (lambda t: t.to(torch.bfloat16) if t.is_floating_point() else t)(torch.as_tensor(a))

    def _A_setup():
        B, H, Sq, Sk, D, M = (2, 32, 1, 4096, 128, 640)
        Dc = 4 * D
        return {'x': _w((lambda *s: torch.randn(*s, dtype=torch.bfloat16) * 0.05)(B, Sq, M)), 'c_kv': _w((lambda *s: torch.randn(*s, dtype=torch.bfloat16) * 1.6)(B, Sk, Dc)), 'w_q': _w((lambda *s: torch.randn(*s, dtype=torch.bfloat16) * 0.8)(M, H, D) * M ** (-0.5)), 'w_uk': _w((lambda *s: torch.randn(*s, dtype=torch.bfloat16) * 0.8)(Dc, H, D) * Dc ** (-0.5)), 'w_uv': _w((lambda *s: torch.randn(*s, dtype=torch.bfloat16) * 1.6)(Dc, H, D) * Dc ** (-0.5)), 'w_o': _w((lambda *s: torch.randn(*s, dtype=torch.bfloat16) * 1.6)(H, D, M) * (H * D) ** (-0.5))}
    _A_in = _A_setup()

    def _B_setup():
        B, S, D, F = (2, 1, 640, 1728)
        return {'x': _w((lambda *s: torch.randn(*s, dtype=torch.bfloat16) * 0.05)(B, S, D)), 'w1': _w((lambda *s: torch.randn(*s, dtype=torch.bfloat16) * 0.05)(D, F)), 'b1': _w((lambda *s: torch.randn(*s, dtype=torch.bfloat16) * 0.2)(F)), 'w2': _w((lambda *s: torch.randn(*s, dtype=torch.bfloat16) * 0.05)(F, D)), 'b2': _w((lambda *s: torch.randn(*s, dtype=torch.bfloat16) * 1.6)(D))}
    _B_in = _B_setup()
    return {'A_x': _A_in['x'], 'A_c_kv': _A_in['c_kv'], 'A_w_q': _A_in['w_q'], 'A_w_uk': _A_in['w_uk'], 'A_w_uv': _A_in['w_uv'], 'A_w_o': _A_in['w_o'], 'B_x': _B_in['x'], 'B_w1': _B_in['w1'], 'B_b1': _B_in['b1'], 'B_w2': _B_in['w2'], 'B_b2': _B_in['b2'], 'c': _w((lambda *s: torch.randn(*s, dtype=torch.bfloat16) * 0.1)(2, 640) * 8.0), 'w_mod': _w((lambda *s: torch.randn(*s, dtype=torch.bfloat16) * 0.1)(640, 6 * 640) * 640 ** (-0.5) * 8.0)}

def reference(A_x, A_c_kv, A_w_q, A_w_uk, A_w_uv, A_w_o, B_x, B_w1, B_b1, B_w2, B_b2, c, w_mod):

    def _A_ref(x, c_kv, w_q, w_uk, w_uv, w_o):
        B, Sq, M = (x.shape[0], x.shape[1], x.shape[2])
        Sk = c_kv.shape[1]
        H, D = (w_q.shape[1], w_q.shape[2])
        q = torch.einsum('bqm,mhd->bhqd', x, w_q)
        k = torch.einsum('bkc,chd->bhkd', c_kv, w_uk)
        v = torch.einsum('bkc,chd->bhkd', c_kv, w_uv)
        scores = torch.einsum('bhqd,bhkd->bhqk', q, k) * D ** (-0.5)
        off = Sk - Sq
        qi = torch.arange(Sq)[:, None]
        kj = torch.arange(Sk)[None, :]
        cmask = kj <= qi + off
        scores = torch.where(cmask[None, None], scores, -1000000000.0)
        p = _softmax(scores, axis=-1)
        ctx = torch.einsum('bhqk,bhkd->bhqd', p, v)
        return torch.einsum('bhqd,hdm->bqm', ctx, w_o)

    def _B_ref(x, w1, b1, w2, b2):
        h = torch.einsum('bsd,df->bsf', x, w1) + b1
        zero = torch.zeros_like(h)
        r = torch.where(h > zero, h, zero)
        h = r * r
        out = torch.einsum('bsf,fd->bsd', h, w2) + b2
        return out
    x = A_x
    mod = (_silu(c) @ w_mod).reshape(c.shape[0], 6, 1, -1)
    sa, ca, ga, sb, cb, gb = (mod[:, 0].clone(), mod[:, 1].clone(), mod[:, 2].clone(), mod[:, 3].clone(), mod[:, 4].clone(), mod[:, 5].clone())
    h = _layernorm(x, axis=-1) * (1 + ca) + sa
    x = x + ga * _A_ref(h, A_c_kv, A_w_q, A_w_uk, A_w_uv, A_w_o)
    h = _layernorm(x, axis=-1) * (1 + cb) + sb
    x = x + gb * _B_ref(h, B_w1, B_b1, B_w2, B_b2)
    return x
```

```cpp
#include <hip/hip_runtime.h>
#include <hip/hip_bf16.h>
#include <stdint.h>

typedef unsigned short u16;
__device__ __forceinline__ float bf2f(u16 b){ return __builtin_bit_cast(float, (unsigned)b << 16); }
__device__ __forceinline__ u16 f2bf(float f){ __hip_bfloat16 h = __float2bfloat16(f); return __builtin_bit_cast(u16, h); }

constexpr int B=2,H=32,SK=4096,D=128,M=640,DC=512,F=1728;

__global__ void k_mod(const u16* c, const u16* wmod, float* mod){
  int j = blockIdx.x*blockDim.x+threadIdx.x; if(j>=6*M) return;
  float a0=0,a1=0;
  for(int k=0;k<M;k++){ float w=bf2f(wmod[k*6*M+j]); float c0=bf2f(c[k]), c1=bf2f(c[M+k]);
    a0+= c0/(1.f+__expf(-c0))*w; a1+= c1/(1.f+__expf(-c1))*w; }
  mod[j]=a0; mod[6*M+j]=a1;
}
__global__ void k_ln(const u16* xb, const float* xf, const float* mod, int sidx, int cidx, float* h){
  int b=blockIdx.x; __shared__ float xs[M]; __shared__ float red[2];
  for(int i=threadIdx.x;i<M;i+=blockDim.x) xs[i]= xb? bf2f(xb[b*M+i]) : xf[b*M+i];
  __syncthreads();
  if(threadIdx.x==0){ float s=0; for(int i=0;i<M;i++) s+=xs[i]; float m=s/M; float v=0; for(int i=0;i<M;i++){float d=xs[i]-m; v+=d*d;} v/=M; red[0]=m; red[1]=1.f/sqrtf(v+1e-5f);}
  __syncthreads();
  for(int i=threadIdx.x;i<M;i+=blockDim.x) h[b*M+i]=(xs[i]-red[0])*red[1]*(1.f+mod[b*6*M+cidx*M+i])+mod[b*6*M+sidx*M+i];
}
__global__ void k_q(const float* h, const u16* wq, float* q){
  int j = blockIdx.x*blockDim.x+threadIdx.x; if(j>=H*D) return;
  float a0=0,a1=0;
  for(int k=0;k<M;k++){ float w=bf2f(wq[k*H*D+j]); a0+=h[k]*w; a1+=h[M+k]*w; }
  q[j]=a0; q[H*D+j]=a1;
}
__global__ void k_qabs(const float* q, const u16* wuk, float* qa){
  int j = blockIdx.x*blockDim.x+threadIdx.x; if(j>=H*DC) return; int h=j/DC, c=j%DC;
  float a0=0,a1=0;
  for(int d=0;d<D;d++){ float w=bf2f(wuk[(c*H+h)*D+d]); a0+=q[h*D+d]*w; a1+=q[H*D+h*D+d]*w; }
  qa[j]=a0; qa[H*DC+j]=a1;
}
__global__ void k_scores(const float* qa, const u16* ckv, float* s){
  int j = blockIdx.x*blockDim.x+threadIdx.x;
  int k=j%SK, h=(j/SK)%H, b=j/(SK*H);
  float a=0; const u16* row=ckv+((size_t)b*SK+k)*DC; const float* qq=qa+(b*H+h)*DC;
  for(int c=0;c<DC;c++) a+=qq[c]*bf2f(row[c]);
  s[j]=a*0.08838834764831845f;
}
__global__ void k_softmax(float* s){
  int bh=blockIdx.x; float* r=s+bh*SK; __shared__ float red[256];
  float m=-1e30f; for(int i=threadIdx.x;i<SK;i+=256) m=fmaxf(m,r[i]); red[threadIdx.x]=m; __syncthreads();
  for(int o=128;o>0;o>>=1){ if(threadIdx.x<o) red[threadIdx.x]=fmaxf(red[threadIdx.x],red[threadIdx.x+o]); __syncthreads(); }
  m=red[0]; __syncthreads();
  float t=0; for(int i=threadIdx.x;i<SK;i+=256){ float e=__expf(r[i]-m); r[i]=e; t+=e;} red[threadIdx.x]=t; __syncthreads();
  for(int o=128;o>0;o>>=1){ if(threadIdx.x<o) red[threadIdx.x]+=red[threadIdx.x+o]; __syncthreads(); }
  float inv=1.f/red[0];
  for(int i=threadIdx.x;i<SK;i+=256) r[i]*=inv;
}
__global__ void k_pv(const float* p, const u16* ckv, float* o){
  int j = blockIdx.x*blockDim.x+threadIdx.x;
  int c=j%DC, h=(j/DC)%H, b=j/(DC*H);
  float a=0; const float* pp=p+(b*H+h)*SK; const u16* col=ckv+(size_t)b*SK*DC+c;
  for(int k=0;k<SK;k++) a+=pp[k]*bf2f(col[(size_t)k*DC]);
  o[j]=a;
}
__global__ void k_ctx(const float* o, const u16* wuv, float* ctx){
  int j = blockIdx.x*blockDim.x+threadIdx.x; if(j>=H*D) return; int h=j/D;
  float a0=0,a1=0;
  for(int c=0;c<DC;c++){ float w=bf2f(wuv[c*H*D+j]); a0+=o[h*DC+c]*w; a1+=o[H*DC+h*DC+c]*w; }
  ctx[j]=a0; ctx[H*D+j]=a1;
}
__global__ void k_wo(const float* ctx, const u16* wo, const u16* x, const float* mod, float* x1){
  int j = blockIdx.x*blockDim.x+threadIdx.x; if(j>=M) return;
  float a0=0,a1=0;
  for(int k=0;k<H*D;k++){ float w=bf2f(wo[k*M+j]); a0+=ctx[k]*w; a1+=ctx[H*D+k]*w; }
  x1[j]=bf2f(x[j])+mod[2*M+j]*a0; x1[M+j]=bf2f(x[M+j])+mod[6*M+2*M+j]*a1;
}
__global__ void k_f1(const float* h, const u16* w1, const u16* b1, float* f){
  int j = blockIdx.x*blockDim.x+threadIdx.x; if(j>=F) return;
  float a0=0,a1=0;
  for(int k=0;k<M;k++){ float w=bf2f(w1[k*F+j]); a0+=h[k]*w; a1+=h[M+k]*w; }
  a0+=bf2f(b1[j]); a1+=bf2f(b1[j]); a0=fmaxf(a0,0.f); a1=fmaxf(a1,0.f);
  f[j]=a0*a0; f[F+j]=a1*a1;
}
__global__ void k_f2(const float* f, const u16* w2, const u16* b2, const float* x1, const float* mod, u16* out){
  int j = blockIdx.x*blockDim.x+threadIdx.x; if(j>=M) return;
  float a0=0,a1=0;
  for(int k=0;k<F;k++){ float w=bf2f(w2[k*M+j]); a0+=f[k]*w; a1+=f[F+k]*w; }
  a0+=bf2f(b2[j]); a1+=bf2f(b2[j]);
  out[j]=f2bf(x1[j]+mod[5*M+j]*a0); out[M+j]=f2bf(x1[M+j]+mod[6*M+5*M+j]*a1);
}

extern "C" void kernel_launch(void* const* d_in, const int* in_sizes, int n_in,
                              void* d_out, int out_size, void* d_ws, size_t ws_size,
                              hipStream_t stream) {
  const u16* x=(const u16*)d_in[0]; const u16* ckv=(const u16*)d_in[1]; const u16* wq=(const u16*)d_in[2];
  const u16* wuk=(const u16*)d_in[3]; const u16* wuv=(const u16*)d_in[4]; const u16* wo=(const u16*)d_in[5];
  const u16* w1=(const u16*)d_in[7]; const u16* b1=(const u16*)d_in[8]; const u16* w2=(const u16*)d_in[9]; const u16* b2=(const u16*)d_in[10];
  const u16* c=(const u16*)d_in[11]; const u16* wmod=(const u16*)d_in[12];
  float* ws=(float*)d_ws;
  float* mod=ws; float* h=mod+2*6*M; float* q=h+2*M; float* qa=q+2*H*D; float* s=qa+2*H*DC; float* o=s+2*H*SK; float* ctx=o+2*H*DC; float* x1=ctx+2*H*D; float* h2=x1+2*M; float* f=h2+2*M;
  k_mod<<<6*M/64,64,0,stream>>>(c,wmod,mod);
  k_ln<<<2,256,0,stream>>>(x,nullptr,mod,0,1,h);
  k_q<<<H*D/64,64,0,stream>>>(h,wq,q);
  k_qabs<<<H*DC/64,64,0,stream>>>(q,wuk,qa);
  k_scores<<<B*H*SK/256,256,0,stream>>>(qa,ckv,s);
  k_softmax<<<B*H,256,0,stream>>>(s);
  k_pv<<<B*H*DC/64,64,0,stream>>>(s,ckv,o);
  k_ctx<<<H*D/64,64,0,stream>>>(o,wuv,ctx);
  k_wo<<<M/64,64,0,stream>>>(ctx,wo,x,mod,x1);
  k_ln<<<2,256,0,stream>>>(nullptr,x1,mod,3,4,h2);
  k_f1<<<F/64,64,0,stream>>>(h2,w1,b1,f);
  k_f2<<<M/64,64,0,stream>>>(f,w2,b2,x1,mod,(u16*)d_out);
}
```

```cpp
#include <hip/hip_runtime.h>
#include <stdint.h>

typedef unsigned short u16;
typedef unsigned v4u __attribute__((ext_vector_type(4)));
typedef unsigned v2u __attribute__((ext_vector_type(2)));
typedef float f32x4 __attribute__((ext_vector_type(4)));
typedef float f32x2 __attribute__((ext_vector_type(2)));
typedef __bf16 bf16x8 __attribute__((ext_vector_type(8)));
typedef __bf16 bf16x2 __attribute__((ext_vector_type(2)));
typedef short s16x4 __attribute__((ext_vector_type(4)));
typedef __amdgpu_buffer_rsrc_t rsrc_t;
#define GAS __attribute__((address_space(1)))
#define LAS __attribute__((address_space(3)))
#define CAS __attribute__((address_space(4)))

constexpr int KV_IDLE = 2;
constexpr int KV7 = 4;
constexpr int PRE15 = 4;
constexpr int D2 = 6;
constexpr int D4 = 24;
constexpr int D6 = 16;
constexpr int CODE_PF_BYTES = 22528;

struct Params {
  const u16 *wq, *wmod, *w1, *c, *x, *wuk;
  unsigned char* ws;
  const u16 *ckv, *wuv, *wo, *w2, *b1, *b2;
  u16* out;
};
typedef unsigned long long u64x8 __attribute__((ext_vector_type(8)));
typedef unsigned long long u64x4 __attribute__((ext_vector_type(4)));
typedef unsigned long long u64x2 __attribute__((ext_vector_type(2)));

constexpr int EPOCH = 0;
constexpr int HB1 = 4096;
constexpr int HB15 = HB1 + 40960;
constexpr int HB2 = HB15 + 131072;
constexpr int HB3S = HB2 + 65536;
constexpr int HB35 = HB3S + 32768;
constexpr int HB4 = HB35 + 131072;
constexpr int HB5 = HB4 + 20480;
constexpr int HB6 = HB5 + 40960;
constexpr int HH2 = HB6 + 276480;
constexpr int HH4 = HH2 + 16384;
constexpr int HCLR = HH4 + 16384;
constexpr int HB3 = 1 << 20;
constexpr int HBEND = HB3 + (2 << 20);
constexpr unsigned EMPTY = 0xffffffffu;
static_assert(HCLR + 1024 <= HB3 && HB5 % 128 == 0 && HB6 % 128 == 0 && HH2 % 128 == 0, "ws map");

constexpr int L_KV = 0, L_Q = 131072, L_P = L_Q + 16384, L_MX = L_P + 4096, L_LS = L_MX + 512, L_END = L_LS + 512;

constexpr int SPIN_MAX = 1 << 17;

__device__ __forceinline__ unsigned f2u(float f) { return __builtin_bit_cast(unsigned, f); }
__device__ __forceinline__ float u2f(unsigned u) { return __builtin_bit_cast(float, u); }
__device__ __forceinline__ float bflo(unsigned u) { return u2f(u << 16); }
__device__ __forceinline__ float bfhi(unsigned u) { return u2f(u & 0xffff0000u); }
__device__ __forceinline__ unsigned pk(float a, float b) { f32x2 v = {a, b}; return __builtin_bit_cast(unsigned, __builtin_convertvector(v, bf16x2)); }
__device__ __forceinline__ unsigned pks(float a, float b) { return min(pk(a, b), EMPTY - 1u); }
__device__ __forceinline__ rsrc_t mkrs(const void* p, unsigned bytes) { return __builtin_amdgcn_make_buffer_rsrc((void*)p, 0, bytes, 0x00020000); }
__device__ __forceinline__ v4u ldw(rsrc_t r, int voff, int soff) { return __builtin_amdgcn_raw_buffer_load_b128(r, voff, soff, 2); }
__device__ __forceinline__ v4u ldh(rsrc_t r, int voff, int soff) { return __builtin_amdgcn_raw_buffer_load_b128(r, voff, soff, 16); }
__device__ __forceinline__ v2u ldh2(rsrc_t r, int voff, int soff) { return __builtin_amdgcn_raw_buffer_load_b64(r, voff, soff, 16); }
__device__ __forceinline__ void sth4(v4u v, rsrc_t r, int voff) { __builtin_amdgcn_raw_buffer_store_b128(v, r, voff, 0, 16); }
__device__ __forceinline__ void sth2(v2u v, rsrc_t r, int voff) { __builtin_amdgcn_raw_buffer_store_b64(v, r, voff, 0, 16); }
__device__ __forceinline__ f32x4 mfma32(v4u a, v4u b, f32x4 c) { return __builtin_amdgcn_mfma_f32_16x16x32_bf16(__builtin_bit_cast(bf16x8, a), __builtin_bit_cast(bf16x8, b), c, 0, 0, 0); }
__device__ __forceinline__ f32x4 mfma16(v2u a, v2u b, f32x4 c) { return __builtin_amdgcn_mfma_f32_16x16x16bf16_1k(__builtin_bit_cast(s16x4, a), __builtin_bit_cast(s16x4, b), c, 0, 0, 0); }
__device__ __forceinline__ unsigned plo(unsigned r0, unsigned r1) { return __builtin_amdgcn_perm(r1, r0, 0x05040100u); }
__device__ __forceinline__ unsigned phi(unsigned r0, unsigned r1) { return __builtin_amdgcn_perm(r1, r0, 0x07060302u); }
template <int CTRL> __device__ __forceinline__ float dppf(float v) { return u2f((unsigned)__builtin_amdgcn_update_dpp(0, (int)f2u(v), CTRL, 0xf, 0xf, true)); }
template <int CTRL> __device__ __forceinline__ unsigned dppu(unsigned v) { return (unsigned)__builtin_amdgcn_update_dpp(0, (int)v, CTRL, 0xf, 0xf, true); }
constexpr int ROR4 = 0x124, ROR8 = 0x128, SHR2 = 0x112, SHR4 = 0x114, SHL4 = 0x104, QX1 = 0xB1, QX2 = 0x4E;
__device__ __forceinline__ float rs32(float a, float b) { auto r = __builtin_amdgcn_permlane32_swap(f2u(a), f2u(b), false, false); unsigned x = r[0], y = r[1]; return u2f(x) + u2f(y); }
__device__ __forceinline__ float rs16(float a, float b) { auto r = __builtin_amdgcn_permlane16_swap(f2u(a), f2u(b), false, false); unsigned x = r[0], y = r[1]; return u2f(x) + u2f(y); }
__device__ __forceinline__ float mx32(float a) { auto r = __builtin_amdgcn_permlane32_swap(f2u(a), f2u(a), false, false); unsigned x = r[0], y = r[1]; return fmaxf(u2f(x), u2f(y)); }
__device__ __forceinline__ float mx16(float a) { auto r = __builtin_amdgcn_permlane16_swap(f2u(a), f2u(a), false, false); unsigned x = r[0], y = r[1]; return fmaxf(u2f(x), u2f(y)); }
__device__ __forceinline__ float sum32(float s) { s += dppf<QX1>(s); s += dppf<QX2>(s); s += dppf<ROR4>(s); s += dppf<ROR8>(s); return rs16(s, s); }
__device__ __forceinline__ float silu(float v) { return v * __builtin_amdgcn_rcpf(1.f + __expf(-v)); }
#define LDSBAR() do { asm volatile("s_waitcnt lgkmcnt(0)" ::: "memory"); __builtin_amdgcn_s_barrier(); asm volatile("" ::: "memory"); } while (0)
#define SB() __builtin_amdgcn_sched_barrier(0)
#define HOT(c) __builtin_expect(!!(c), 1)
#define Z4 (f32x4{0.f, 0.f, 0.f, 0.f})

__device__ __forceinline__ void dma16(rsrc_t r, int voff, int soff, int lds) {
  unsigned keep;
  asm volatile("s_mov_b32 %0, m0\n\ts_mov_b32 m0, %4\n\ts_nop 0\n\tbuffer_load_dwordx4 %1, %2, %3 offen nt lds\n\ts_mov_b32 m0, %0"
               : "=&s"(keep) : "v"(voff), "s"(r), "s"(soff), "s"(lds) : "memory");
}
template <int N> __device__ __forceinline__ bool tags_bad(const v4u (&v)[N], unsigned tag) {
  unsigned bad = 0;
#pragma unroll
  for (int k = 0; k < N; ++k) bad |= (v[k].y ^ tag) | (v[k].w ^ tag);
  return __any(bad != 0);
}
__device__ __forceinline__ void xpose8(const v4u (&L)[8], v4u (&F)[8]) {
#pragma unroll
  for (int p = 0; p < 4; ++p) {
    F[2 * p] = v4u{plo(L[0][p], L[1][p]), plo(L[2][p], L[3][p]), plo(L[4][p], L[5][p]), plo(L[6][p], L[7][p])};
    F[2 * p + 1] = v4u{phi(L[0][p], L[1][p]), phi(L[2][p], L[3][p]), phi(L[4][p], L[5][p]), phi(L[6][p], L[7][p])};
  }
  asm volatile("" : "+v"(F[0]), "+v"(F[1]), "+v"(F[2]), "+v"(F[3]), "+v"(F[4]), "+v"(F[5]), "+v"(F[6]), "+v"(F[7]));
}
template <int N, int SLEEP, typename SO>
__device__ __forceinline__ bool poll(rsrc_t rs, int voff, SO so, unsigned tag, v4u (&v)[N], bool act, int vstep = 0) {
#pragma unroll
  for (int k = 0; k < N; ++k) v[k] = v4u{0u, tag, 0u, tag};
  bool ok = !act;
  int spin;
#pragma unroll 1
  for (spin = 0; spin < SPIN_MAX; ++spin) {
    if (!ok) {
#pragma unroll
      for (int k = 0; k < N; ++k) v[k] = ldh(rs, voff + k * vstep, so(k));
    }
    unsigned bad = 0;
#pragma unroll
    for (int k = 0; k < N; ++k) bad |= (v[k].y ^ tag) | (v[k].w ^ tag);
    ok = bad == 0;
    if (__all(ok)) break;
    if (SLEEP) __builtin_amdgcn_s_sleep(SLEEP);
    asm volatile("" ::: "memory");
  }
  asm volatile("" ::: "memory");
  return spin != 0;
}
template <int N> __device__ __forceinline__ bool has_empty(const v4u (&v)[N]) {
  unsigned m = 0;
#pragma unroll
  for (int k = 0; k < N; ++k) m = max(max(m, v[k].x), max(max(v[k].y, v[k].z), v[k].w));
  return m == EMPTY;
}
template <int N> __device__ __forceinline__ bool has_empty(const v2u (&v)[N]) {
  unsigned m = 0;
#pragma unroll
  for (int k = 0; k < N; ++k) m = max(m, max(v[k].x, v[k].y));
  return m == EMPTY;
}
__device__ __forceinline__ void ldx(v4u& v, rsrc_t r, int voff, int soff) { v = ldh(r, voff, soff); }
__device__ __forceinline__ void ldx(v2u& v, rsrc_t r, int voff, int soff) { v = ldh2(r, voff, soff); }
template <int N, int SLEEP, typename V, typename SO>
__device__ __forceinline__ void repoll(rsrc_t rs, int voff, SO so, V (&v)[N], bool act) {
#pragma unroll 1
  for (int spin = 0; spin < SPIN_MAX; ++spin) {
    const bool bad = act && has_empty(v);
    if (!__any(bad)) break;
    if (SLEEP) __builtin_amdgcn_s_sleep(SLEEP);
    asm volatile("" ::: "memory");
    if (bad) {
#pragma unroll
      for (int k = 0; k < N; ++k) ldx(v[k], rs, voff, so(k));
    }
  }
  asm volatile("" ::: "memory");
}

__device__ __forceinline__ void put2(LAS unsigned char* d, const f32x4 (&a)[8]) {
  *(LAS f32x4*)(d) = f32x4{a[0][0], a[1][0], a[2][0], a[3][0]};
  *(LAS f32x4*)(d + 16) = f32x4{a[4][0], a[5][0], a[6][0], a[7][0]};
  *(LAS f32x4*)(d + 512) = f32x4{a[0][1], a[1][1], a[2][1], a[3][1]};
  *(LAS f32x4*)(d + 528) = f32x4{a[4][1], a[5][1], a[6][1], a[7][1]};
}
template <int N, typename V> __device__ __forceinline__ V lds_sum(LAS unsigned char* a, int stride) {
  V v[N];
#pragma unroll
  for (int k = 0; k < N; ++k) v[k] = *(LAS V*)(a + k * stride);
  SB();
#pragma unroll
  for (int d = 1; d < N; d *= 2)
#pragma unroll
    for (int k = 0; k + d < N; k += 2 * d) v[k] += v[k + d];
  return v[0];
}
#define PINV(x) asm volatile("" : "+v"(x))
#define UNDEF8(a) asm volatile("" : "=v"(a[0]), "=v"(a[1]), "=v"(a[2]), "=v"(a[3]), "=v"(a[4]), "=v"(a[5]), "=v"(a[6]), "=v"(a[7]))

__global__ void __launch_bounds__(512, 2) fused_decode_kernel(Params pk_) {
  __shared__ __attribute__((aligned(16))) unsigned char smem[L_END];
  LAS unsigned char* L = (LAS unsigned char*)smem;
  u64x8 ka0; u64x4 ka1; u64x2 ka2;
  asm volatile("s_load_dwordx16 %0, %3, 0x0\n\ts_load_dwordx8 %1, %3, 0x40\n\ts_load_dwordx4 %2, %3, 0x60"
               : "=&s"(ka0), "=&s"(ka1), "=&s"(ka2) : "s"(__builtin_amdgcn_kernarg_segment_ptr()));
  SB();
  const int tid = threadIdx.x, lane = tid & 63, w = __builtin_amdgcn_readfirstlane(tid >> 6);
  const int i = lane & 15, g = lane >> 4;
  const int blk = blockIdx.x, x = blk & 7, r = blk >> 3, rr = r & 15;
  unsigned pfv = 0u;
  if (w == 7) {
    unsigned long long pc;
    asm volatile("s_getpc_b64 %0" : "=s"(pc));
    const int o = r * 1024 + lane * 128;
    if (lane < 8 && o < CODE_PF_BYTES) pfv = *(const GAS unsigned*)((pc & ~255ull) + o);
  }
  const int h = 4 * x + (rr >> 2), qd = rr & 3;
  const int u1 = rr * 8 + x, mc = (u1 * 37) >> 8, p1 = u1 - 7 * mc;
  const int bb = x >> 2, kc = (x & 3) * 8 + (rr >> 1), hg = rr & 1;
  const bool isA = r < 16, s1 = !isA && u1 < 70;
  asm volatile("s_waitcnt lgkmcnt(0)" : "+s"(ka0), "+s"(ka1), "+s"(ka2));
  Params p;
  p.wq = (const u16*)ka0[0]; p.wmod = (const u16*)ka0[1]; p.w1 = (const u16*)ka0[2]; p.c = (const u16*)ka0[3]; p.x = (const u16*)ka0[4]; p.wuk = (const u16*)ka0[5];
  p.ws = (unsigned char*)ka0[6]; p.ckv = (const u16*)ka0[7]; p.wuv = (const u16*)ka1[0]; p.wo = (const u16*)ka1[1]; p.w2 = (const u16*)ka1[2]; p.b1 = (const u16*)ka1[3];
  p.b2 = (const u16*)ka2[0]; p.out = (u16*)ka2[1];
  unsigned tag;
  const rsrc_t rws = mkrs(p.ws, HBEND);
  const v4u E4 = {EMPTY, EMPTY, EMPTY, EMPTY};
  const int o3 = HB3 + ((((16 * hg + (lane >> 3)) * 4 + (w >> 1)) * 2 + bb) * 32 + kc) * 256 + (w & 1) * 128 + (lane & 7) * 16;
  auto load_kv = [&]() {
    sth4(E4, rws, o3);
    sth4(E4, rws, o3 + 8 * 65536);
    SB();
    const rsrc_t rkv = mkrs(p.ckv, 2 * 4096 * 1024);
    const int kvrow = (bb * 4096 + kc * 128 + 16 * w) << 10;
#pragma unroll 1
    for (int j = 0; j < 16; ++j) {
      const int jr = (j + 8 * hg) & 15;
      dma16(rkv, (lane ^ jr) << 4, kvrow + jr * 1024, L_KV + (16 * w + jr) * 1024);
    }
  };
#define TAG_READY() asm volatile("s_load_dword %0, %1, 0x0\n\ts_waitcnt lgkmcnt(0)\n\ts_add_u32 %0, %0, 1" : "=&s"(tag) : "s"(p.ws) : "scc")
  v4u WL[8];
  UNDEF8(WL);
  if (isA) {
    if (w < 5) {
      const rsrc_t rq = mkrs(p.wq, 640 * 8192);
#pragma unroll
      for (int j = 0; j < 8; ++j) WL[j] = ldw(rq, g * 65536 + i * 16, (160 * qd + 32 * w + j) * 8192 + h * 256);
    }
    const rsrc_t rk = mkrs(p.wuk, 512 * 8192);
#pragma unroll
    for (int k = 0; k < 4; ++k)
      dma16(rk, g * 8192 + ((i ^ (4 * k + g)) << 4), (128 * qd + 16 * w) * 8192 + h * 256 + k * 32768, 32768 + w * 4096 + k * 1024);
  } else if (w == 6) {
    const rsrc_t rx = mkrs(p.x, 2560);
#pragma unroll
    for (int k = 0; k < 5; ++k) { const v2u t = __builtin_amdgcn_raw_buffer_load_b64(rx, (lane >> 5) * 1280 + (lane & 31) * 8, k * 256, 0); WL[k].x = t.x; WL[k].y = t.y; }
    WL[5].x = __builtin_amdgcn_raw_buffer_load_b32(rx, (lane >> 5) * 1280 + (lane & 31) * 4, mc * 128, 0);
    WL[6].x = __builtin_amdgcn_raw_buffer_load_b32(rx, (lane >> 5) * 1280, 0, 0);
  }
  SB();
  int s1o = (r >> 4) & ((u1 - 70) >> 31);
  asm volatile("" : "+s"(s1o));
  if (HOT(s1o)) {
    if (HOT(w < 6)) {
      const rsrc_t rm = mkrs(p.wmod, 640 * 7680), rc = mkrs(p.c, 2560);
      const v2u cv = __builtin_amdgcn_raw_buffer_load_b64(rc, (i & 1) * 1280 + g * 8, (96 * p1 + 16 * w) * 2, 0);
      v4u W[4];
#pragma unroll
      for (int j = 0; j < 4; ++j) W[j] = ldw(rm, g * 30720 + (i >> 3) * 1280 + (i & 7) * 16, (96 * p1 + 16 * w + j) * 7680 + mc * 128);
      SB();
      v2u A = {pk(silu(bflo(cv.x)), silu(bfhi(cv.x))), pk(silu(bflo(cv.y)), silu(bfhi(cv.y)))};
      int a2 = L_Q + w * 1024 + lane * 32;
      asm volatile("" : "+v"(A), "+v"(a2));
      f32x4 a[8];
#pragma unroll
      for (int t = 0; t < 8; ++t) {
        const int q = t >> 1;
        const v2u B = (t & 1) ? v2u{phi(W[0][q], W[1][q]), phi(W[2][q], W[3][q])} : v2u{plo(W[0][q], W[1][q]), plo(W[2][q], W[3][q])};
        a[t] = mfma16(A, B, Z4);
      }
      if (lane < 16) put2(L + a2, a);
      LDSBAR();
      TAG_READY();
    } else if (w == 6) {
      const int b = lane >> 5, l5 = lane & 31;
      const v2u xv[5] = {v2u{WL[0].x, WL[0].y}, v2u{WL[1].x, WL[1].y}, v2u{WL[2].x, WL[2].y}, v2u{WL[3].x, WL[3].y}, v2u{WL[4].x, WL[4].y}};
      const unsigned xm = WL[5].x, x0 = WL[6].x;
      TAG_READY();
      const float c0 = bflo(x0);
      float s = 0.f, q = 0.f;
#pragma unroll
      for (int k = 0; k < 5; ++k) {
        const float d0 = bflo(xv[k].x) - c0, d1 = bfhi(xv[k].x) - c0, d2 = bflo(xv[k].y) - c0, d3 = bfhi(xv[k].y) - c0;
        s += (d0 + d1) + (d2 + d3);
        q += (d0 * d0 + d1 * d1) + (d2 * d2 + d3 * d3);
      }
      s += dppf<QX1>(s); q += dppf<QX1>(q); s += dppf<QX2>(s); q += dppf<QX2>(q);
      s += dppf<ROR4>(s); q += dppf<ROR4>(q); s += dppf<ROR8>(s); q += dppf<ROR8>(q);
      s = rs16(s, s) * (1.f / 640.f);
      q = rs16(q, q) * (1.f / 640.f);
      const float rstd = __builtin_amdgcn_rsqf(fmaxf(q - s * s, 0.f) + 1e-5f), mu = c0 + s;
      float n0 = (bflo(xm) - mu) * rstd, n1 = (bfhi(xm) - mu) * rstd;
      float e0 = p1 == 0 ? n0 : 0.f, e1 = p1 == 0 ? n1 : 0.f;
      int a0 = L_Q + b * 512 + l5 * 8, a1 = HB1 + (p1 * 2 + b) * 2560 + mc * 256 + l5 * 8;
      asm volatile("" : "+v"(n0), "+v"(n1), "+v"(e0), "+v"(e1), "+v"(a0), "+v"(a1));
      LDSBAR();
      f32x2 sa[6], ca[6];
#pragma unroll
      for (int k = 0; k < 6; ++k) { sa[k] = *(LAS f32x2*)(L + a0 + k * 1024); ca[k] = *(LAS f32x2*)(L + a0 + k * 1024 + 256); }
      SB();
      const f32x2 sas = ((sa[0] + sa[1]) + (sa[2] + sa[3])) + (sa[4] + sa[5]), cas = ((ca[0] + ca[1]) + (ca[2] + ca[3])) + (ca[4] + ca[5]);
      sth2(v2u{pk(n0 * cas[0] + (sas[0] + e0), n1 * cas[1] + (sas[1] + e1)), tag}, rws, a1);
    } else {
      __builtin_amdgcn_s_sleep(KV7);
      load_kv();
      TAG_READY();
      __builtin_amdgcn_s_barrier();
    }
  } else {
    TAG_READY();
  }
  SB();
  int groupB = r >> 4;
  asm volatile("" : "+s"(groupB));

  if (!groupB) {
    const int nf = x < 3 ? 4 : 3, fi = (x < 3 ? 4 * x : 3 * x + 3) + rr - 10;
    const int cg = (x < 3 ? 2 * x : 3 * x - 3) + rr - 10 - nf;
    const int cu5 = 2 * rr + (x & 1), p5 = x >> 1;
    if (w == 5) {
      if (lane < 32) sth4(E4, rws, HB2 + (g * 32 + h) * 1024 + qd * 256 + i * 16);
    } else if (w == 6) {
      if (rr < 10) {
        if (lane < 16) sth4(E4, rws, HB4 + (x * 2 + (i >> 3)) * 1280 + rr * 128 + (i & 7) * 16);
        if (lane < 32) sth4(E4, rws, HB5 + (p5 * 2 + g) * 5120 + cu5 * 256 + i * 16);
      }
    }
    asm volatile("s_waitcnt vmcnt(0)" ::: "memory");
    v4u WK[4];
#pragma unroll
    for (int s = 0; s < 4; ++s) WK[s] = *(LAS v4u*)(L + 32768 + w * 4096 + i * 256 + (((4 * s + g) ^ i) << 4));
    if (HOT(w < 5)) {
      v4u F[8];
      xpose8(WL, F);
      int a0 = 65536 + w * 4096 + ((lane >> 3) & 1) * 64 + (lane & 7) * 8 + g * 2, a1 = 65536 + w * 4096 + (i & 1) * 64 + g * 16, a2 = w * 1024 + lane * 32;
      PINV(a0); PINV(a1); PINV(a2);
      v4u hv[2];
      poll<2, 1>(rws, HB1 + (lane >> 3) * 2560 + (5 * qd + w) * 128 + (lane & 7) * 16, [&](int k) { return 0; }, tag, hv, true, lane < 48 ? 20480 : 0);
      if (lane >= 48) { hv[1].x = 0u; hv[1].z = 0u; }
      const float u0 = rs32(bflo(hv[0].x) + bflo(hv[1].x), bflo(hv[0].z) + bflo(hv[1].z));
      const float u1_ = rs32(bfhi(hv[0].x) + bfhi(hv[1].x), bfhi(hv[0].z) + bfhi(hv[1].z));
      *(LAS u16*)(L + a0) = (u16)pk(rs16(u0, u1_), 0.f);
      const v4u A = *(LAS v4u*)(L + a1);
      f32x4 a[8];
#pragma unroll
      for (int t = 0; t < 8; ++t) a[t] = mfma32(A, F[t], Z4);
      if (lane < 16) put2(L + a2, a);
    }
    LDSBAR();
    if (HOT(w == 7)) {
      const f32x4 s = lds_sum<5, f32x4>(L + lane * 16, 1024);
      sth4(v4u{pk(s[0], s[1]), tag, pk(s[2], s[3]), tag}, rws, HB15 + (h * 4 + qd) * 1024 + lane * 16);
    } else if (w == 6) {
      if (lane < 8) sth4(v4u{0u, tag, 0u, tag}, rws, HH2 + (h * 4 + qd) * 128 + lane * 16);
      if (lane == 0) sth2(v2u{0u, tag}, rws, HCLR + (h * 4 + qd) * 8);
    }
    if (HOT(w < 4)) {
      v4u qv[1];
      int a0 = 8192 + ((lane >> 3) & 3) * 512 + (2 * w + (lane >> 5)) * 64 + (lane & 7) * 8;
      PINV(a0);
      __builtin_amdgcn_s_sleep(PRE15);
      poll<1, 1>(rws, HB15 + (h * 4 + w) * 1024 + lane * 16, [](int) { return 0; }, tag, qv, true);
      *(LAS v2u*)(L + a0) = v2u{qv[0].x, qv[0].z};
    }
    LDSBAR();
    {
      f32x4 a = Z4;
      v4u QB[4];
#pragma unroll
      for (int s = 0; s < 4; ++s) QB[s] = *(LAS v4u*)(L + 8192 + s * 512 + (i & 7) * 64 + g * 16);
      SB();
#pragma unroll
      for (int s = 0; s < 4; ++s) a = mfma32(WK[s], QB[s], a);
      const float sc = 0.08838834764831845f * 1.4426950408889634f;
      float o[4];
#pragma unroll
      for (int k = 0; k < 4; ++k) {
        float t = a[k];
        t += dppf<SHR2>(t);
        t += dppf<SHR4>(t);
        o[k] = t * sc;
      }
      if (i == 6 || i == 7) *(LAS v2u*)(L + 12288 + (i & 1) * 256 + w * 32 + g * 8) = v2u{pks(o[0], o[1]), pks(o[2], o[3])};
    }
    LDSBAR();
    if (HOT(w == 7)) {
      if (lane < 32) sth4(*(LAS v4u*)(L + 12288 + lane * 16), rws, HB2 + (g * 32 + h) * 1024 + qd * 256 + i * 16);
    } else {
      __builtin_amdgcn_s_sleep(2);
    }
    SB();
    if (rr < 10) {
      if (HOT(w < 4)) {
        const rsrc_t ro = mkrs(p.wo, 4096 * 1280);
        const int vo = (512 * x + 128 * w + 8 * g) * 1280 + rr * 128 + i * 8;
        v2u WO[4][8];
#pragma unroll
        for (int s = 0; s < 4; ++s)
#pragma unroll
          for (int j = 0; j < 8; ++j) WO[s][j] = __builtin_amdgcn_raw_buffer_load_b64(ro, vo, (32 * s + j) * 1280, 2);
        SB();
        v4u FO[4][4];
#pragma unroll
        for (int s = 0; s < 4; ++s) {
          FO[s][0] = v4u{plo(WO[s][0].x, WO[s][1].x), plo(WO[s][2].x, WO[s][3].x), plo(WO[s][4].x, WO[s][5].x), plo(WO[s][6].x, WO[s][7].x)};
          FO[s][1] = v4u{phi(WO[s][0].x, WO[s][1].x), phi(WO[s][2].x, WO[s][3].x), phi(WO[s][4].x, WO[s][5].x), phi(WO[s][6].x, WO[s][7].x)};
          FO[s][2] = v4u{plo(WO[s][0].y, WO[s][1].y), plo(WO[s][2].y, WO[s][3].y), plo(WO[s][4].y, WO[s][5].y), plo(WO[s][6].y, WO[s][7].y)};
          FO[s][3] = v4u{phi(WO[s][0].y, WO[s][1].y), phi(WO[s][2].y, WO[s][3].y), phi(WO[s][4].y, WO[s][5].y), phi(WO[s][6].y, WO[s][7].y)};
          asm volatile("" : "+v"(FO[s][0]), "+v"(FO[s][1]), "+v"(FO[s][2]), "+v"(FO[s][3]));
        }
        int a0 = 65536 + w * 4096 + ((lane >> 3) & 3) * 512 + (lane >> 5) * 64 + (lane & 7) * 8, a1 = 65536 + w * 4096 + (i & 7) * 64 + g * 16, a2 = 24576 + w * 1024 + g * 512 + i * 16;
        PINV(a0); PINV(a1); PINV(a2);
        __builtin_amdgcn_s_barrier();
        v4u cx[4];
        poll<4, 1>(rws, HB35 + (4 * x + w) * 4096 + lane * 16, [](int k) { return k * 1024; }, tag, cx, true);
#pragma unroll
        for (int k = 0; k < 4; ++k) *(LAS v2u*)(L + a0 + k * 128) = v2u{cx[k].x, cx[k].z};
        v4u A[4];
#pragma unroll
        for (int s = 0; s < 4; ++s) A[s] = *(LAS v4u*)(L + a1 + s * 512);
        SB();
        f32x4 a[4] = {Z4, Z4, Z4, Z4};
#pragma unroll
        for (int s = 0; s < 4; ++s)
#pragma unroll
          for (int t = 0; t < 4; ++t) a[t] = mfma32(A[s], FO[s][t], a[t]);
        if (lane < 32) {
          *(LAS f32x4*)(L + a2) = f32x4{a[0][0] + a[0][2], a[1][0] + a[1][2], a[2][0] + a[2][2], a[3][0] + a[3][2]};
          *(LAS f32x4*)(L + a2 + 256) = f32x4{a[0][1] + a[0][3], a[1][1] + a[1][3], a[2][1] + a[2][3], a[3][1] + a[3][3]};
        }
        LDSBAR();
      } else {
        {
          const rsrc_t rm = mkrs(p.wmod, 640 * 7680);
          const int k0 = 160 * p5 + 40 * (w - 4);
          v4u W3[2];
          unsigned cw[5];
#pragma unroll
          for (int j = 0; j < 5; ++j) cw[j] = *(const GAS unsigned*)(p.c + (i & 1) * 640 + k0 + 10 * g + 2 * j);
#pragma unroll
          for (int j = 0; j < 8; ++j) WL[j] = ldw(rm, g * 76800 + i * 16, (k0 + j) * 7680 + 2560 + cu5 * 256);
#pragma unroll
          for (int j = 0; j < 2; ++j) W3[j] = ldw(rm, g * 76800 + i * 16, (k0 + 8 + j) * 7680 + 2560 + cu5 * 256);
          SB();
          unsigned sw[5];
#pragma unroll
          for (int j = 0; j < 5; ++j) sw[j] = pk(silu(bflo(cw[j])), silu(bfhi(cw[j])));
          v4u F[8];
          xpose8(WL, F);
          f32x4 a[8];
#pragma unroll
          for (int t = 0; t < 8; ++t) {
            a[t] = mfma32(v4u{sw[0], sw[1], sw[2], sw[3]}, F[t], Z4);
            const unsigned b2 = (t & 1) ? phi(W3[0][t >> 1], W3[1][t >> 1]) : plo(W3[0][t >> 1], W3[1][t >> 1]);
            a[t] = mfma32(v4u{sw[4], 0u, 0u, 0u}, v4u{b2, 0u, 0u, 0u}, a[t]);
          }
          if (lane < 16) put2(L + 16384 + (w - 4) * 1024 + lane * 32, a);
          LDSBAR();
          if (w < 6) {
            const f32x2 s = lds_sum<4, f32x2>(L + 16384 + (w - 4) * 512 + lane * 8, 1024);
            __builtin_amdgcn_raw_buffer_store_b32(pks(s[0], s[1]), rws, HB5 + (p5 * 2 + (w - 4)) * 5120 + cu5 * 256 + lane * 4, 0, 16);
          }
        }
        if (w == 7) {
          int a0 = 24576 + (lane >> 5) * 2048 + (lane & 31) * 16, a1 = HB4 + (x * 2 + g) * 1280 + rr * 128 + i * 8;
          PINV(a0); PINV(a1);
          __builtin_amdgcn_s_barrier();
          asm volatile("" ::: "memory");
          const f32x4 s = lds_sum<4, f32x4>(L + a0, 512);
          const float c0 = rs32(s[0], s[0]), c1 = rs32(s[1], s[1]), c2 = rs32(s[2], s[2]), c3 = rs32(s[3], s[3]);
          if (lane < 32) sth2(v2u{pks(c0, c1), pks(c2, c3)}, rws, a1);
        }
      }
    } else if (rr - 10 < nf) {
      if (w < 5) {
        const int b = g >> 1, m0 = 128 * w + 8 * i + 4 * (g & 1);
        const rsrc_t r1 = mkrs(p.w1, 640 * 3456), r2 = mkrs(p.w2, 1728 * 1280);
        const int fo = 64 * fi + 32 * (i >> 3) + 2 * g + ((i >> 2) & 1);
        const v2u xv = *(const GAS v2u*)(p.x + b * 640 + m0);
        const unsigned cxu = *(const GAS u16*)(p.x + b * 640);
        unsigned b1u[4];
#pragma unroll
        for (int k = 0; k < 4; ++k) b1u[k] = *(const GAS u16*)(p.b1 + fo + 8 * k);
        v4u cl[1], F1[2][8], F2[2][8];
        cl[0] = ldh(rws, HCLR + lane * 16, 0);
        {
          v4u WA[4][8];
#pragma unroll
          for (int t = 0; t < 2; ++t)
#pragma unroll
            for (int j = 0; j < 8; ++j) WA[t][j] = ldw(r1, (32 * (i >> 3) + 8 * g) * 3456 + (i & 7) * 16, (128 * w + 64 * t + j) * 3456 + fi * 128);
#pragma unroll
          for (int t = 0; t < 2; ++t)
#pragma unroll
            for (int j = 0; j < 8; ++j) WA[2 + t][j] = ldw(r2, g * 2560 + i * 16, (64 * fi + 32 * t + 8 * (j & 3) + (j >> 2)) * 1280 + w * 256);
          SB();
          xpose8(WA[0], F1[0]);
          SB();
          xpose8(WA[1], F1[1]);
          SB();
          xpose8(WA[2], F2[0]);
          SB();
          xpose8(WA[3], F2[1]);
        }
        __builtin_amdgcn_s_barrier();
        if (tags_bad(cl, tag)) poll<1, 4>(rws, HCLR + lane * 16, [](int) { return 0; }, tag, cl, true);
        asm volatile("" ::: "memory");
        v2u mv[12];
#pragma unroll
        for (int k = 0; k < 12; ++k) mv[k] = ldh2(rws, HB5 + b * 5120 + m0 * 2, (k / 3) * 10240 + (k % 3) * 1280);
        repoll<12, 4>(rws, HB5 + b * 5120 + m0 * 2, [](int k) { return (k / 3) * 10240 + (k % 3) * 1280; }, mv, true);
        const float cx = bflo(cxu);
        float xx[4] = {bflo(xv.x) - cx, bfhi(xv.x) - cx, bflo(xv.y) - cx, bfhi(xv.y) - cx};
        float ga[4], sb[4], cb1[4];
#pragma unroll
        for (int k = 0; k < 4; ++k) {
          auto el = [&](int q) { const unsigned u = (k & 2) ? mv[q].y : mv[q].x; return (k & 1) ? bfhi(u) : bflo(u); };
          ga[k] = (el(0) + el(3)) + (el(6) + el(9));
          sb[k] = (el(1) + el(4)) + (el(7) + el(10));
          cb1[k] = 1.f + (el(2) + el(5)) + (el(8) + el(11));
        }
        float b1v[4] = {bflo(b1u[0]), bflo(b1u[1]), bflo(b1u[2]), bflo(b1u[3])};
        int tw = 98304 + w * 2048 + b * 256 + i * 16 + (g & 1) * 8;
        int tr = 98304 + w * 2048 + (i >> 2) * 512 + (i & 1) * 256 + ((i >> 1) & 1) * 64 + g * 16;
        int rw = 110592 + w * 3264 + i * 272 + (g & 1) * 128;
        int ro = 110592 + (i & 1) * 544 + ((i >> 1) & 1) * 272 + (i >> 3) * 128 + g * 32 + ((i >> 2) & 1) * 16;
        int sw = 131072 + b * 128 + (2 * w + (g & 1)) * 8, sr = 131072 + ((i >> 1) & 1) * 128 + (i & 1) * 32, sr2 = 131072 + ((i >> 1) & 1) * 128 + 64;
        int vo = HB4 + g * 1280 + w * 256 + i * 16, so = HB6 + (((4 * w + (i >> 2)) * 27 + fi) * 2 + (g >> 1)) * 128 + (i & 3) * 32 + (g & 1) * 16;
        const unsigned one = ((i >> 3) == (g & 1)) ? ((i & 1) ? 0x3f800000u : 0x00003f80u) : 0u;
        v4u SEL = {((i >> 1) & 3) == 0 ? one : 0u, ((i >> 1) & 3) == 1 ? one : 0u, ((i >> 1) & 3) == 2 ? one : 0u, ((i >> 1) & 3) == 3 ? one : 0u};
        *(LAS v2u*)(L + tw + 512) = v2u{pk(cb1[0], cb1[1]), pk(cb1[2], cb1[3])};
        *(LAS v2u*)(L + tw + 1024) = v2u{pk(sb[0], sb[1]), pk(sb[2], sb[3])};
        *(LAS v2u*)(L + tw + 1536) = v2u{0u, 0u};
        asm volatile("" : "+v"(SEL), "+v"(tw), "+v"(tr), "+v"(rw), "+v"(ro), "+v"(sw), "+v"(sr), "+v"(sr2), "+v"(vo), "+v"(so));
        asm volatile("" : "+v"(xx[0]), "+v"(xx[1]), "+v"(xx[2]), "+v"(xx[3]), "+v"(ga[0]), "+v"(ga[1]), "+v"(ga[2]), "+v"(ga[3]), "+v"(cb1[0]), "+v"(cb1[1]), "+v"(cb1[2]), "+v"(cb1[3]),
                     "+v"(b1v[0]), "+v"(b1v[1]), "+v"(b1v[2]), "+v"(b1v[3]));
        v4u av[4], hv[1];
        if (poll<1, 2>(rws, HH4 + (lane & 31) * 512 + (w & 3) * 128, [](int) { return 0; }, tag, hv, true)) __builtin_amdgcn_s_sleep(D4);
#pragma unroll
        for (int k = 0; k < 4; ++k) av[k] = ldh(rws, vo, k * 5120);
        repoll<4, 1>(rws, vo, [](int k) { return k * 5120; }, av, true);
        f32x4 a = Z4;
#pragma unroll
        for (int k = 0; k < 4; ++k) a = mfma32(SEL, av[k], a);
#pragma unroll
        for (int k = 0; k < 4; ++k) a[k] = xx[k] + ga[k] * a[k];
        *(LAS v2u*)(L + tw) = v2u{pk(a[0] * cb1[0], a[1] * cb1[1]), pk(a[2] * cb1[2], a[3] * cb1[3])};
        const v4u B0 = *(LAS v4u*)(L + tr), B1 = *(LAS v4u*)(L + tr + 128);
        SB();
        float s1_ = (a[0] + a[1]) + (a[2] + a[3]), s2 = (a[0] * a[0] + a[1] * a[1]) + (a[2] * a[2] + a[3] * a[3]);
        s1_ += dppf<QX1>(s1_); s2 += dppf<QX1>(s2); s1_ += dppf<QX2>(s1_); s2 += dppf<QX2>(s2);
        SB();
        f32x4 c[8];
#pragma unroll
        for (int t = 0; t < 8; ++t) c[t] = mfma32(F1[0][t], B0, Z4);
        s1_ += dppf<ROR4>(s1_); s2 += dppf<ROR4>(s2); s1_ += dppf<ROR8>(s1_); s2 += dppf<ROR8>(s2);
        if (i == 0) *(LAS f32x2*)(L + sw) = f32x2{s1_, s2};
#pragma unroll
        for (int t = 0; t < 8; ++t) c[t] = mfma32(F1[1][t], B1, c[t]);
        if (((i >> 1) & 1) == (g >> 1) && i < 12) {
#pragma unroll
          for (int t = 0; t < 8; ++t) *(LAS f32x4*)(L + rw + t * 16) = c[t];
        }
        LDSBAR();
        f32x4 pu[5], pg[5], ps[5];
        const f32x4 y0 = *(LAS f32x4*)(L + sr), y1 = *(LAS f32x4*)(L + sr + 16), y2 = *(LAS f32x4*)(L + sr2);
        SB();
#pragma unroll
        for (int k = 0; k < 5; ++k) pg[k] = *(LAS f32x4*)(L + ro + k * 3264 + 1088);
#pragma unroll
        for (int k = 0; k < 5; ++k) pu[k] = *(LAS f32x4*)(L + ro + k * 3264);
#pragma unroll
        for (int k = 0; k < 5; ++k) ps[k] = *(LAS f32x4*)(L + ro + k * 3264 + 2176);
        SB();
        f32x4 y = y0 + y1;
        PINV(y);
        s1_ = y[0] + y[2]; s2 = y[1] + y[3];
        s1_ += dppf<QX1>(s1_); s2 += dppf<QX1>(s2);
        const float mu = (s1_ + (y2[0] + y2[2])) * (1.f / 640.f);
        const float rstd = __builtin_amdgcn_rsqf(fmaxf((s2 + (y2[1] + y2[3])) * (1.f / 640.f) - mu * mu, 0.f) + 1e-5f);
        f32x4 G = (pg[0] + pg[1]) + (pg[2] + pg[3]) + pg[4];
        PINV(G);
        f32x4 U = (pu[0] + pu[1]) + (pu[2] + pu[3]) + pu[4] - mu * G;
        PINV(U);
        f32x4 S = (ps[0] + ps[1]) + (ps[2] + ps[3]) + ps[4] + rstd * U;
        PINV(S);
        float f[4];
#pragma unroll
        for (int k = 0; k < 4; ++k) {
          float t = S[k];
          t += dppf<QX1>(t);
          t = fmaxf(t + b1v[k], 0.f);
          f[k] = t * t;
        }
        const unsigned f0 = pk(f[0], f[1]), f1 = pk(f[2], f[3]);
        const v4u A0 = {f0, f1, dppu<SHL4>(f0), dppu<SHL4>(f1)};
        const v4u A1 = {dppu<ROR8>(A0.x), dppu<ROR8>(A0.y), dppu<ROR8>(A0.z), dppu<ROR8>(A0.w)};
        f32x4 o[8];
#pragma unroll
        for (int t = 0; t < 8; ++t) o[t] = mfma32(A0, F2[0][t], Z4);
#pragma unroll
        for (int t = 0; t < 8; ++t) o[t] = mfma32(A1, F2[1][t], o[t]);
        const unsigned q0 = pk(o[0][0], o[1][0]), q1 = pk(o[2][0], o[3][0]), q2 = pk(o[4][0], o[5][0]), q3 = pk(o[6][0], o[7][0]);
        const unsigned q4 = pk(o[0][2], o[1][2]), q5 = pk(o[2][2], o[3][2]), q6 = pk(o[4][2], o[5][2]), q7 = pk(o[6][2], o[7][2]);
        auto e0 = __builtin_amdgcn_permlane16_swap(q0, q2, false, false), e1 = __builtin_amdgcn_permlane16_swap(q1, q3, false, false);
        auto e2 = __builtin_amdgcn_permlane16_swap(q4, q6, false, false), e3 = __builtin_amdgcn_permlane16_swap(q5, q7, false, false);
        const unsigned e00 = e0[0], e10 = e1[0], e20 = e2[0], e30 = e3[0];
        auto d0 = __builtin_amdgcn_permlane32_swap(e00, e20, false, false), d1 = __builtin_amdgcn_permlane32_swap(e10, e30, false, false);
        const unsigned d00 = d0[0], d10 = d1[0];
        sth4(v4u{d00, tag, d10, tag}, rws, so);
      }
    } else if (cg < 20) {
      float base[4], gbv[4];
      if (w == 0) {
        const int b = (lane >> 3) & 1, m0 = 32 * cg + 4 * (lane & 7);
        const v2u xv = *(const GAS v2u*)(p.x + b * 640 + m0), bv = *(const GAS v2u*)(p.b2 + m0);
        v4u hv[1];
        v2u mv[8], av[8];
        poll<1, 4>(rws, HCLR + lane * 16, [](int) { return 0; }, tag, hv, true);
        if (poll<1, 4>(rws, HH4 + lane * 256, [](int) { return 0; }, tag, hv, true)) __builtin_amdgcn_s_sleep(D4 + 4);
#pragma unroll
        for (int k = 0; k < 8; ++k) mv[k] = ldh2(rws, HB5 + b * 5120 + m0 * 2, (k >> 1) * 10240 + (k & 1) * 3840);
#pragma unroll
        for (int k = 0; k < 8; ++k) av[k] = ldh2(rws, HB4 + b * 1280 + m0 * 2, k * 2560);
        repoll<8, 2>(rws, HB4 + b * 1280 + m0 * 2, [](int k) { return k * 2560; }, av, lane < 16);
        __builtin_amdgcn_s_barrier();
        if (cg == 0 && lane == 0) __hip_atomic_store((GAS unsigned*)(p.ws + EPOCH), tag, __ATOMIC_RELAXED, __HIP_MEMORY_SCOPE_AGENT);
        repoll<8, 2>(rws, HB5 + b * 5120 + m0 * 2, [](int k) { return (k >> 1) * 10240 + (k & 1) * 3840; }, mv, lane < 16);
        float a[4] = {0.f, 0.f, 0.f, 0.f};
#pragma unroll
        for (int k = 0; k < 8; ++k) { a[0] += bflo(av[k].x); a[1] += bfhi(av[k].x); a[2] += bflo(av[k].y); a[3] += bfhi(av[k].y); }
        const float xx[4] = {bflo(xv.x), bfhi(xv.x), bflo(xv.y), bfhi(xv.y)}, b2v[4] = {bflo(bv.x), bfhi(bv.x), bflo(bv.y), bfhi(bv.y)};
#pragma unroll
        for (int k = 0; k < 4; ++k) {
          auto el = [&](int q) { const unsigned u = (k & 2) ? mv[q].y : mv[q].x; return (k & 1) ? bfhi(u) : bflo(u); };
          gbv[k] = (el(1) + el(3)) + (el(5) + el(7));
          base[k] = xx[k] + ((el(0) + el(2)) + (el(4) + el(6))) * a[k] + gbv[k] * b2v[k];
        }
        asm volatile("" : "+v"(base[0]), "+v"(base[1]), "+v"(base[2]), "+v"(base[3]), "+v"(gbv[0]), "+v"(gbv[1]), "+v"(gbv[2]), "+v"(gbv[3]));
      } else {
        v4u ov[1];
        int a0 = 24576 + w * 256 + i * 16 + g * 4;
        PINV(a0);
        __builtin_amdgcn_s_barrier();
        asm volatile("" ::: "memory");
        __builtin_amdgcn_s_sleep(D6);
        poll<1, 1>(rws, HB6 + cg * 6912 + (w - 1) * 1024 + lane * 16, [](int) { return 0; }, tag, ov, (w - 1) * 64 + lane < 432);
        *(LAS float*)(L + a0) = rs16(rs32(bflo(ov[0].x), bflo(ov[0].z)), rs32(bfhi(ov[0].x), bfhi(ov[0].z)));
      }
      LDSBAR();
      if (HOT(w == 0 && lane < 16)) {
        const f32x4 s = lds_sum<7, f32x4>(L + 24576 + 256 + lane * 16, 256);
        const v2u o = {pk(base[0] + gbv[0] * s[0], base[1] + gbv[1] * s[1]), pk(base[2] + gbv[2] * s[2], base[3] + gbv[3] * s[3])};
        __builtin_amdgcn_raw_buffer_store_b64(o, mkrs(p.out, 2560), (lane >> 3) * 1280 + cg * 64 + (lane & 7) * 8, 0, 0);
      }
    }
  } else {
    const bool kv_done = s1 && w == 7;
    {
      const int nsl = s1 ? 0 : KV_IDLE;
      if (!kv_done) {
        __builtin_amdgcn_s_sleep(3);
#pragma unroll 1
        for (int k = 0; k < nsl; ++k) __builtin_amdgcn_s_sleep(8);
        load_kv();
      }
    }
    const int ho = HH2 + (16 * hg + 2 * w) * 512 + (lane & 7) * 128, qo = HB2 + (bb * 32 + 16 * hg + 2 * w) * 1024 + lane * 16;
    v4u hv[1], qv[2];
    hv[0] = ldh(rws, ho, 0);
    asm volatile("s_waitcnt vmcnt(0)" ::: "memory");
    const bool qearly = !tags_bad(hv, tag);
    qv[0] = E4;
    qv[1] = E4;
    asm volatile("" ::: "memory");
    if (qearly) {
      qv[0] = ldh(rws, qo, 0);
      qv[1] = ldh(rws, qo, 1024);
    }
    SB();
    v4u SF[16];
    {
      const int pi = 8 * (i >> 3) + 2 * (i & 3) + ((i >> 2) & 1);
      const int base = L_KV + (16 * w + pi) * 1024 + 256 * g;
#pragma unroll
      for (int s = 0; s < 16; ++s) SF[s] = *(LAS v4u*)(L + base + ((s ^ pi) << 4));
    }
    LDSBAR();
    v4u VF[4][4];
    auto load_vf = [&](int k0) {
      const int q = i >> 2, pp = i & 3;
#pragma unroll
      for (int u = 0; u < 4; ++u)
#pragma unroll
        for (int hf = 0; hf < 2; ++hf) {
          const int k15 = 8 * (g & 1) + 2 * q + hf;
          const int a0 = L_KV + (16 * (g >> 1) + k15) * 1024 + (((8 * w + 2 * u + (pp >> 1)) ^ k15) << 4) + 8 * (pp & 1);
#pragma unroll
          for (int ks = k0; ks < k0 + 2; ++ks) {
            const v2u t = __builtin_bit_cast(v2u, __builtin_amdgcn_ds_read_tr16_b64_v4i16((LAS s16x4*)(L + a0 + ks * 32768)));
            VF[ks][u][2 * hf] = t.x;
            VF[ks][u][2 * hf + 1] = t.y;
          }
        }
    };
    load_vf(0);
    load_vf(2);
    {
      if (!qearly) {
        if (poll<1, 2>(rws, ho, [](int) { return 0; }, tag, hv, lane < 8)) __builtin_amdgcn_s_sleep(D2);
        qv[0] = ldh(rws, qo, 0);
        qv[1] = ldh(rws, qo, 1024);
      }
      repoll<2, 1>(rws, qo, [](int k) { return k * 1024; }, qv, true);
#pragma unroll
      for (int u = 0; u < 2; ++u) *(LAS v4u*)(L + L_Q + (2 * w + u) * 1024 + ((lane ^ (2 * w + u)) << 4)) = qv[u];
    }
    LDSBAR();
    f32x4 sc = Z4;
    {
      v4u QF[8];
#pragma unroll
      for (int s = 0; s < 8; ++s) QF[s] = *(LAS v4u*)(L + L_Q + i * 1024 + 256 * g + ((s ^ i) << 4));
      SB();
#pragma unroll
      for (int s = 0; s < 8; ++s) {
        sc = mfma32(SF[s], QF[s], sc);
        QF[s] = *(LAS v4u*)(L + L_Q + i * 1024 + 256 * g + (((s + 8) ^ i) << 4));
        SB();
      }
#pragma unroll
      for (int s = 0; s < 8; ++s) sc = mfma32(SF[s + 8], QF[s], sc);
    }
    SB();
    v4u UV[4];
    {
      const rsrc_t rv = mkrs(p.wuv, 512 * 8192);
#pragma unroll
      for (int j = 0; j < 4; ++j) UV[j] = ldw(rv, g * 32768 + i * 16, (128 * qd + 16 * w + j) * 8192 + h * 256);
    }
    SB();
    {
      float m = fmaxf(fmaxf(sc[0], sc[1]), fmaxf(sc[2], sc[3]));
      m = mx32(mx16(m));
      if (g == 0) *(LAS float*)(L + L_MX + i * 32 + w * 4) = m;
    }
    LDSBAR();
    float M;
    {
      const f32x4 a = *(LAS f32x4*)(L + L_MX + i * 32), b = *(LAS f32x4*)(L + L_MX + i * 32 + 16);
      M = fmaxf(fmaxf(fmaxf(a[0], a[1]), fmaxf(a[2], a[3])), fmaxf(fmaxf(b[0], b[1]), fmaxf(b[2], b[3])));
      const float p0 = __builtin_amdgcn_exp2f(sc[0] - M), p1_ = __builtin_amdgcn_exp2f(sc[1] - M), p2 = __builtin_amdgcn_exp2f(sc[2] - M), p3 = __builtin_amdgcn_exp2f(sc[3] - M);
      *(LAS v2u*)(L + L_P + (((w >> 1) * 4 + 2 * (w & 1) + (g >> 1)) * 16 + i) * 16 + 8 * (g & 1)) = v2u{pk(p0, p1_), pk(p2, p3)};
      float l = (p0 + p1_) + (p2 + p3);
      l = rs16(l, l);
      l = rs32(l, l);
      if (g == 0) *(LAS float*)(L + L_LS + i * 32 + w * 4) = l;
    }
    LDSBAR();
    v4u PF[4];
#pragma unroll
    for (int ks = 0; ks < 4; ++ks) PF[ks] = *(LAS v4u*)(L + L_P + ks * 1024 + lane * 16);
    SB();
    if (HOT(w == 7 && g == 0)) {
      const f32x4 a = *(LAS f32x4*)(L + L_LS + i * 32), b = *(LAS f32x4*)(L + L_LS + i * 32 + 16);
      const float l = ((a[0] + a[1]) + (a[2] + a[3])) + ((b[0] + b[1]) + (b[2] + b[3]));
      sth4(v4u{f2u(M), tag, f2u(l), tag}, rws, HB3S + (bb * 32 + kc) * 512 + hg * 256 + i * 16);
    }
    {
      f32x4 a[4] = {Z4, Z4, Z4, Z4};
#pragma unroll
      for (int ks = 0; ks < 4; ++ks)
#pragma unroll
        for (int u = 0; u < 4; ++u) a[u] = mfma32(VF[ks][u], PF[ks], a[u]);
      LAS unsigned char* T = L + L_Q + w * 2048;
#pragma unroll
      for (int u = 0; u < 4; ++u) *(LAS v2u*)(T + i * 128 + (((2 * u + (g >> 1)) ^ (i & 7)) << 4) + (g & 1) * 8) = v2u{pks(a[u][0], a[u][1]), pks(a[u][2], a[u][3])};
#pragma unroll
      for (int k = 0; k < 2; ++k) sth4(*(LAS v4u*)(T + (8 * k + (lane >> 3)) * 128 + (((lane ^ (lane >> 3)) & 7) << 4)), rws, o3 + k * 8 * 65536);
    }
    SB();
    {
      v2u FU[8];
#pragma unroll
      for (int q = 0; q < 4; ++q) {
        FU[2 * q] = v2u{plo(UV[0][q], UV[1][q]), plo(UV[2][q], UV[3][q])};
        FU[2 * q + 1] = v2u{phi(UV[0][q], UV[1][q]), phi(UV[2][q], UV[3][q])};
      }
      asm volatile("" : "+v"(FU[0]), "+v"(FU[1]), "+v"(FU[2]), "+v"(FU[3]), "+v"(FU[4]), "+v"(FU[5]), "+v"(FU[6]), "+v"(FU[7]));
      int vo = HB3 + (((h * 4 + qd) * 2 + (w >> 2)) * 32 + 8 * (w & 3) + g) * 256 + i * 16;
      int a0 = 1024 + w * 512 + i * 32 + g * 16, a1 = 1024 + (i & 1) * 2048 + w * 64 + g * 16, a2 = 8192 + w * 1024 + lane * 32;
      PINV(vo); PINV(a0); PINV(a1); PINV(a2);
      v4u sv4[1];
      if (w == 0) poll<1, 1>(rws, HB3S + lane * 512 + h * 16, [](int) { return 0; }, tag, sv4, true);
      __builtin_amdgcn_s_barrier();
      asm volatile("" ::: "memory");
      v4u ov[2];
      ov[0] = ldh(rws, vo, 0);
      ov[1] = ldh(rws, vo, 1024);
      SB();
      if (w == 0) {
        const float mk = u2f(sv4[0].x);
        float m = mk;
        m = fmaxf(m, dppf<QX1>(m)); m = fmaxf(m, dppf<QX2>(m)); m = fmaxf(m, dppf<ROR4>(m)); m = fmaxf(m, dppf<ROR8>(m)); m = mx16(m);
        const float e = __builtin_amdgcn_exp2f(mk - m);
        const float l = sum32(e * u2f(sv4[0].z));
        *(LAS unsigned*)(L + lane * 4) = pk(e * __builtin_amdgcn_rcpf(l), 0.f);
      }
      LDSBAR();
      v4u A0, A1;
      {
        const unsigned w0 = *(LAS unsigned*)(L + (8 * w + g) * 4) << (16 * (i & 1)), w1 = *(LAS unsigned*)(L + (8 * w + 4 + g) * 4) << (16 * (i & 1));
#pragma unroll
        for (int k = 0; k < 4; ++k) { A0[k] = (i >> 1) == k ? w0 : 0u; A1[k] = (i >> 1) == k ? w1 : 0u; }
        asm volatile("" : "+v"(A0), "+v"(A1));
      }
      repoll<2, 1>(rws, vo, [](int k) { return k * 1024; }, ov, true);
      const f32x4 o = mfma32(A1, ov[1], mfma32(A0, ov[0], Z4));
      if (lane < 32) *(LAS f32x4*)(L + a0) = o;
      LDSBAR();
      SB();
      const f32x4 c = lds_sum<4, f32x4>(L + a1, 512);
      const v2u A = {pk(c[0], c[1]), pk(c[2], c[3])};
      f32x4 a[8];
#pragma unroll
      for (int t = 0; t < 8; ++t) a[t] = mfma16(A, FU[t], Z4);
      if (lane < 16) put2(L + a2, a);
      LDSBAR();
      if (HOT(w == 7)) {
        const f32x4 s = lds_sum<8, f32x4>(L + 8192 + lane * 16, 1024);
        sth4(v4u{pk(s[0], s[1]), tag, pk(s[2], s[3]), tag}, rws, HB35 + (h * 4 + qd) * 1024 + lane * 16);
      } else if (w == 6) {
        if (lane < 8) sth4(v4u{0u, tag, 0u, tag}, rws, HH4 + (h * 4 + qd) * 128 + lane * 16);
      }
    }
  }
  asm volatile("" ::"v"(pfv));
}

extern "C" void kernel_launch(void* const* d_in, const int* in_sizes, int n_in, void* d_out, int out_size, void* d_ws, size_t ws_size, hipStream_t stream) {
  Params p;
  p.x = (const u16*)d_in[0]; p.ckv = (const u16*)d_in[1]; p.wq = (const u16*)d_in[2]; p.wuk = (const u16*)d_in[3];
  p.wuv = (const u16*)d_in[4]; p.wo = (const u16*)d_in[5]; p.w1 = (const u16*)d_in[7]; p.b1 = (const u16*)d_in[8];
  p.w2 = (const u16*)d_in[9]; p.b2 = (const u16*)d_in[10]; p.c = (const u16*)d_in[11]; p.wmod = (const u16*)d_in[12];
  p.out = (u16*)d_out; p.ws = (unsigned char*)d_ws;
  fused_decode_kernel<<<256, 512, 0, stream>>>(p);
}
```
